```python
import torch

def _softmax(x, axis=-1):
    return torch.softmax(x, dim=axis)
def _gelu(x):
    return torch.nn.functional.gelu(x, approximate="tanh")
def _silu(x):
    return torch.nn.functional.silu(x)
def _sigmoid(x):
    return torch.sigmoid(x)
def _layernorm(x, axis=-1, eps=1e-5):
    m = x.mean(dim=axis, keepdim=True)
    v = x.var(dim=axis, keepdim=True, unbiased=False)
    return (x - m) / torch.sqrt(v + eps)
def _scatter_add(tgt, idx, src, axis=0):
    return tgt.clone().index_add_(axis, idx, src)
def _segment_sum(src, seg, n):
    out = torch.zeros((n,) + src.shape[1:], dtype=src.dtype)
    return out.index_add_(0, seg, src)
def _pad(x, pw):
    flat = tuple(v for pair in reversed(pw) for v in pair)
    return torch.nn.functional.pad(x, flat)

kernel_name = 'sequential_preLN__ssm_conv_s4d__moe_arch_mod__prompt'

def setup_inputs():

    _w = (lambda a: (lambda t: t.to(torch.bfloat16) if t.is_floating_point() else t)(torch.as_tensor(a)))
    def _A_setup():
        Bsz, L, D, N = 1, 2048, 640, 192
        return {
            "u": _w((lambda *s: torch.randn(*s, dtype=torch.bfloat16) * 0.05)(Bsz, L, D)),
            "log_A_real": _w((lambda *s: torch.randn(*s, dtype=torch.bfloat16) * 6.4)(D, N) * 0.5),
            "A_imag": _w((lambda *s: torch.randn(*s, dtype=torch.bfloat16) * 0.05)(D, N) * 3.14),
            "C_real": _w((lambda *s: torch.randn(*s, dtype=torch.bfloat16) * 0.05)(D, N) / N ** 0.5),
            "C_imag": _w((lambda *s: torch.randn(*s, dtype=torch.bfloat16) * 0.05)(D, N) / N ** 0.5),
            "log_dt": _w((lambda *s: torch.randn(*s, dtype=torch.bfloat16) * 0.05)(D) * 0.5 - 4.0),
            "D_skip": _w((lambda *s: torch.randn(*s, dtype=torch.bfloat16) * 0.05)(D)),
        }
    _A_in = _A_setup()

    def _B_setup():
        B, S, D, F, C = 1, 2048, 640, 1536, 8
        return {
            "x": _w((lambda *s: torch.randn(*s, dtype=torch.bfloat16) * 0.05)(B, S, D)),
            "w_r": _w((lambda *s: torch.randn(*s, dtype=torch.bfloat16) * 0.05)(D)),
            "w_gate": _w((lambda *s: torch.randn(*s, dtype=torch.bfloat16) * 0.05)(D, F)),
            "w_up": _w((lambda *s: torch.randn(*s, dtype=torch.bfloat16) * 0.05)(D, F)),
            "w_down": _w((lambda *s: torch.randn(*s, dtype=torch.bfloat16) * 0.05)(F, D)),
        }
    _B_in = _B_setup()

    return {
        "A_u": _A_in["u"],
        "A_log_A_real": _A_in["log_A_real"],
        "A_A_imag": _A_in["A_imag"],
        "A_C_real": _A_in["C_real"],
        "A_C_imag": _A_in["C_imag"],
        "A_log_dt": _A_in["log_dt"],
        "A_D_skip": _A_in["D_skip"],
        "B_x": _B_in["x"],
        "B_w_r": _B_in["w_r"],
        "B_w_gate": _B_in["w_gate"],
        "B_w_up": _B_in["w_up"],
        "B_w_down": _B_in["w_down"],
    }

def reference(A_u, A_log_A_real, A_A_imag, A_C_real, A_C_imag, A_log_dt, A_D_skip, B_x, B_w_r, B_w_gate, B_w_up, B_w_down):
    def _A_ref(u, log_A_real, A_imag, C_real, C_imag, log_dt, D_skip):
        L = u.shape[1]
        dt = torch.exp(log_dt)
        A = -torch.exp(log_A_real) + 1j * A_imag
        C = (C_real + 1j * C_imag) * dt[:, None]
        pos = (lambda x, dt: x.to(dt))(torch.arange(L), log_dt.dtype)
        P = torch.exp((A * dt[:, None])[:, :, None] * pos[None, None, :])
        K = 2.0 * torch.einsum("dn,dnl->dl", C, P).real
        L2 = 2 * L
        u_t = (lambda x, *d: x.permute(*d))(u, 0, 2, 1)
        Kf = (lambda x, *a, **k: torch.fft.rfft(x.float(), *a, **k))(K, L2)
        Uf = (lambda x, *a, **k: torch.fft.rfft(x.float(), *a, **k))(u_t, L2)
        y = (lambda x, *a, **k: torch.fft.irfft(x, *a, **k).to(torch.bfloat16))(Uf * Kf[None, :, :], L2)[:, :, :L]
        y = (lambda x, *d: x.permute(*d))(y, 0, 2, 1)
        return y + u * D_skip[None, None, :]

    def _B_ref(x, w_r, w_gate, w_up, w_down):
        B, S, D = x.shape
        C = 8
        r = x @ w_r
        idx = torch.argsort(-r, axis=-1)[:, :C]
        idx_e = (lambda x, n, axis: torch.repeat_interleave(x, n, dim=axis))(idx.reshape(B, C, 1), D, axis=2)
        x_sel = (lambda x, i, axis: torch.take_along_dim(x, i, dim=axis))(x, idx_e, axis=1)
        g = _silu(x_sel @ w_gate)
        u = x_sel @ w_up
        y = (g * u) @ w_down
        r_sel = _sigmoid((lambda x, i, axis: torch.take_along_dim(x, i, dim=axis))(r, idx, axis=1))
        y = y * r_sel.reshape(B, C, 1)
        b_off = (torch.arange(B) * S).reshape(B, 1)
        flat_idx = (idx + b_off).reshape(B * C)
        zero = (x * 0.0).reshape(B * S, D)
        upd = _scatter_add(zero, flat_idx, y.reshape(B * C, D), axis=0)
        return x + upd.reshape(B, S, D)

    x = A_u
    h = x + _A_ref(_layernorm(x, axis=-1), A_log_A_real, A_A_imag, A_C_real, A_C_imag, A_log_dt, A_D_skip)
    h2 = h + _B_ref(_layernorm(h, axis=-1), B_w_r, B_w_gate, B_w_up, B_w_down)
    return h2
```

```cpp
#include <hip/hip_runtime.h>
#include <stdint.h>

namespace {

typedef _Float16 h2 __attribute__((ext_vector_type(2)));
typedef _Float16 h8 __attribute__((ext_vector_type(8)));
typedef __bf16 b2 __attribute__((ext_vector_type(2)));
typedef __bf16 b8 __attribute__((ext_vector_type(8)));
typedef float f2 __attribute__((ext_vector_type(2)));
typedef float f4 __attribute__((ext_vector_type(4)));
typedef unsigned v2u __attribute__((ext_vector_type(2)));
typedef unsigned v4u __attribute__((ext_vector_type(4)));
typedef short s4 __attribute__((ext_vector_type(4)));
typedef unsigned short u16;
#define LAS __attribute__((address_space(3)))

constexpr int SEQ = 2048, DM = 640, NM = 192, FF = 1536;
constexpr int NT = 512, NWG = 256;
constexpr int NG = 214;
constexpr int F1_0 = NG;
constexpr int F2_0 = NG + 32;

constexpr unsigned WS_EPOCH = 0;
constexpr unsigned WS_FLAG = 1024;
constexpr unsigned WS_R = 4096;
constexpr unsigned WS_HID = WS_R + 256 * 64;
constexpr unsigned XS_STRIDE = 2688;
constexpr unsigned WS_XS = WS_HID + 192 * 128;
constexpr unsigned WS_U = WS_XS + 8 * XS_STRIDE;
constexpr unsigned WS_Y = WS_U + NG * 256 * 64;
constexpr unsigned WS_END = WS_Y + 256 * NG * 64;
constexpr unsigned WS_DBG = 16u << 20;

constexpr unsigned L_CTL = 0;
constexpr unsigned C_SUB = 0;
constexpr unsigned C_CNT = 16;
constexpr unsigned C_DUMP = 20;
constexpr unsigned C_R8 = 32;
constexpr unsigned C_GMAX = 48;
constexpr unsigned C_SC = 80;
constexpr unsigned L_ROWS = 256;
constexpr unsigned ROW_DW = 648;
constexpr unsigned L_U = L_ROWS + 4 * ROW_DW * 4;
constexpr unsigned U_CH = 4640;
constexpr unsigned L_W = L_U + 3 * U_CH;
constexpr unsigned W_CH = 1296;
constexpr unsigned L_TAB = L_W + 3 * W_CH + 16;
constexpr unsigned TAB_ROW = 2368;
constexpr unsigned KC_COPY = 4160, KC_CH = 4 * KC_COPY;
constexpr unsigned L_PART = L_TAB + 3 * KC_CH;
constexpr unsigned L_P = L_PART;
constexpr unsigned L_Y = L_PART + 24576;
constexpr unsigned Y_CH = 5120;
static_assert(L_PART + 36864 <= L_Y + 3 * Y_CH, "part");
constexpr unsigned L_XS = L_ROWS + 4 * ROW_DW * 4;
constexpr unsigned XS_ROW = 1312;
constexpr unsigned L_STG = L_ROWS;
constexpr unsigned L_CAND = L_XS + 8 * XS_ROW;
constexpr unsigned L_FW = L_CAND + 8192;
constexpr unsigned FW_IMG = 640 * 96;
constexpr unsigned L_RED = L_ROWS;
constexpr unsigned L_SP = L_FW + 2 * FW_IMG;
constexpr unsigned C_TS = L_SP + 9216;
constexpr unsigned L_END = C_TS + 256;
static_assert(L_TAB % 16 == 0 && L_PART % 16 == 0 && L_FW % 16 == 0 && L_U % 16 == 0 && L_W % 16 == 0 && L_CAND % 16 == 0, "align");
static_assert(L_Y + 3 * Y_CH <= C_TS && L_FW + 6 * 16384 <= C_TS && L_RED + 24576 <= L_FW, "lds map");
static_assert(L_END <= 160 * 1024, "lds");
static_assert(L_STG + 8 * 2560 <= L_CAND, "stg");

constexpr unsigned SENT = 0xFFFFFFFFu;
#define SPINCAP (1 << 16)
constexpr int SPIN_CAP = SPINCAP;
#define DLY_S2 48
#define DLY_S3 24
#define DLY_XS 22
#define DLY_HID 40
#define OUT_AUX 16
#define RS2 1
#define DLY_PROBE 4
#define DLY_S2B 40

template <int V> struct IC { static constexpr int value = V; };

struct Params {
    const u16* x; const u16* lA; const u16* Aim; const u16* Cr; const u16* Ci; const u16* ldt; const u16* Dsk;
    const u16* wr; const u16* wg; const u16* wu; const u16* wd; u16* out; char* ws;
};

__device__ __forceinline__ float bflo(unsigned w) { return __uint_as_float(w << 16); }
__device__ __forceinline__ float bfhi(unsigned w) { return __uint_as_float(w & 0xffff0000u); }
__device__ __forceinline__ unsigned pkbf(float a, float b) { return __builtin_bit_cast(unsigned, __builtin_convertvector(f2{a, b}, b2)); }
__device__ __forceinline__ unsigned pkh(float a, float b) { return __builtin_bit_cast(unsigned, __builtin_convertvector(f2{a, b}, h2)); }
__device__ __forceinline__ float rbf(float a) { return bflo(pkbf(a, a)); }
__device__ __forceinline__ unsigned nosent(unsigned w) { return w == SENT ? 0x7FC07FC0u : w; }

__device__ __forceinline__ f2 cmulf(f2 a, f2 b) {
    f2 t, r;
    asm("v_pk_mul_f32 %0, %1, %2 op_sel_hi:[0,1]" : "=v"(t) : "v"(a), "v"(b));
    asm("v_pk_fma_f32 %0, %1, %2, %3 op_sel:[1,1,0] op_sel_hi:[1,0,1] neg_lo:[1,0,0]" : "=v"(r) : "v"(a), "v"(b), "v"(t));
    return r;
}
struct FF2 { f2 a, b; };
__device__ __forceinline__ FF2 cmulf2(f2 a0, f2 b0, f2 a1, f2 b1) {
    f2 r0, r1;
    asm("v_pk_mul_f32 %0, %2, %3 op_sel_hi:[0,1]\n\t"
        "v_pk_mul_f32 %1, %4, %5 op_sel_hi:[0,1]\n\t"
        "v_pk_fma_f32 %0, %2, %3, %0 op_sel:[1,1,0] op_sel_hi:[1,0,1] neg_lo:[1,0,0]\n\t"
        "v_pk_fma_f32 %1, %4, %5, %1 op_sel:[1,1,0] op_sel_hi:[1,0,1] neg_lo:[1,0,0]"
        : "=&v"(r0), "=&v"(r1) : "v"(a0), "v"(b0), "v"(a1), "v"(b1));
    return FF2{r0, r1};
}
__device__ __forceinline__ v4u cmulh4(v4u a, v4u b) {
    unsigned r0, r1, r2, r3;
    asm("v_pk_mul_f16 %0, %4, %8 op_sel_hi:[0,1]\n\t"
        "v_pk_mul_f16 %1, %5, %9 op_sel_hi:[0,1]\n\t"
        "v_pk_mul_f16 %2, %6, %10 op_sel_hi:[0,1]\n\t"
        "v_pk_mul_f16 %3, %7, %11 op_sel_hi:[0,1]\n\t"
        "v_pk_fma_f16 %0, %4, %8, %0 op_sel:[1,1,0] op_sel_hi:[1,0,1] neg_lo:[1,0,0]\n\t"
        "v_pk_fma_f16 %1, %5, %9, %1 op_sel:[1,1,0] op_sel_hi:[1,0,1] neg_lo:[1,0,0]\n\t"
        "v_pk_fma_f16 %2, %6, %10, %2 op_sel:[1,1,0] op_sel_hi:[1,0,1] neg_lo:[1,0,0]\n\t"
        "v_pk_fma_f16 %3, %7, %11, %3 op_sel:[1,1,0] op_sel_hi:[1,0,1] neg_lo:[1,0,0]"
        : "=&v"(r0), "=&v"(r1), "=&v"(r2), "=&v"(r3)
        : "v"(a.x), "v"(a.y), "v"(a.z), "v"(a.w), "v"(b.x), "v"(b.y), "v"(b.z), "v"(b.w));
    return v4u{r0, r1, r2, r3};
}
__device__ __forceinline__ unsigned cmulh(unsigned a, unsigned b) {
    unsigned t, r;
    asm("v_pk_mul_f16 %0, %1, %2 op_sel_hi:[0,1]" : "=v"(t) : "v"(a), "v"(b));
    asm("v_pk_fma_f16 %0, %1, %2, %3 op_sel:[1,1,0] op_sel_hi:[1,0,1] neg_lo:[1,0,0]" : "=v"(r) : "v"(a), "v"(b), "v"(t));
    return r;
}
template <int CTRL, int RM>
__device__ __forceinline__ float dppf(float v) { return __builtin_bit_cast(float, __builtin_amdgcn_update_dpp(0, __builtin_bit_cast(int, v), CTRL, RM, 0xf, true)); }
template <int CTRL, int RM>
__device__ __forceinline__ unsigned dppu(unsigned v) { return (unsigned)__builtin_amdgcn_update_dpp(0, (int)v, CTRL, RM, 0xf, true); }
__device__ __forceinline__ float wave_sum(float v) {
    v += dppf<0x111, 0xf>(v); v += dppf<0x112, 0xf>(v); v += dppf<0x114, 0xf>(v); v += dppf<0x118, 0xf>(v);
    v += dppf<0x142, 0xa>(v); v += dppf<0x143, 0xc>(v);
    return __builtin_bit_cast(float, __builtin_amdgcn_readlane(__builtin_bit_cast(int, v), 63));
}
__device__ __forceinline__ unsigned umax(unsigned a, unsigned b) { return a > b ? a : b; }
__device__ __forceinline__ unsigned half_max(unsigned v) {
    v = umax(v, dppu<0x111, 0xf>(v)); v = umax(v, dppu<0x112, 0xf>(v)); v = umax(v, dppu<0x114, 0xf>(v)); v = umax(v, dppu<0x118, 0xf>(v));
    v = umax(v, dppu<0x142, 0xa>(v));
    return v;
}
__device__ __forceinline__ unsigned wave_max(unsigned v) {
    v = half_max(v); v = umax(v, dppu<0x143, 0xc>(v));
    return (unsigned)__builtin_amdgcn_readlane((int)v, 63);
}

#define BAR() do { asm volatile("s_waitcnt lgkmcnt(0)" ::: "memory"); __builtin_amdgcn_s_barrier(); asm volatile("" ::: "memory"); } while (0)
#define VMWAIT() asm volatile("s_waitcnt vmcnt(0)" ::: "memory")
#define LGWAIT() asm volatile("s_waitcnt lgkmcnt(0)" ::: "memory")
#define LD_SC1(rs, off) __builtin_amdgcn_raw_buffer_load_b128(rs, off, 0, 16)
#define ST_SC1(v, rs, off) __builtin_amdgcn_raw_buffer_store_b128(v, rs, off, 0, 16)
#define LDS(T, a) (*(LAS T*)(size_t)(a))
#define LDSV(T, a) (*(LAS volatile T*)(size_t)(a))

#define STAMP(i, w) do { } while (0)
#define CSTAMP(i, w) do { } while (0)

template <class R>
__device__ __forceinline__ void dma16(R rsrc, unsigned voff, unsigned soff, unsigned lds_dst) {
    asm volatile("s_nop 4\n\ts_mov_b32 m0, %0\n\ts_nop 0\n\tbuffer_load_dwordx4 %1, %2, %3 offen lds" :: "s"(lds_dst), "v"(voff), "s"(rsrc), "s"(soff) : "memory");
}
__device__ __forceinline__ unsigned long long quad_or(unsigned long long m) {
    m = (m | (m >> 1) | (m >> 2) | (m >> 3)) & 0x1111111111111111ull;
    return m * 15ull;
}

__device__ __forceinline__ f2 cexp_at(f2 z, float p) {
    const f2 t = z * p;
    const float m = __builtin_amdgcn_exp2f(t.x);
    const float a = __builtin_amdgcn_fractf(t.y);
    return f2{__builtin_amdgcn_cosf(a), __builtin_amdgcn_sinf(a)} * m;
}
__device__ __forceinline__ f2 cexp_at(f2 z, f2 p) {
    const f2 t = z * p;
    const float m = __builtin_amdgcn_exp2f(t.x);
    const float a = __builtin_amdgcn_fractf(t.y);
    return f2{__builtin_amdgcn_cosf(a), __builtin_amdgcn_sinf(a)} * m;
}
__device__ __forceinline__ void wave_sum2(float& a, float& c) {
    a += dppf<0x111, 0xf>(a); c += dppf<0x111, 0xf>(c); a += dppf<0x112, 0xf>(a); c += dppf<0x112, 0xf>(c);
    a += dppf<0x114, 0xf>(a); c += dppf<0x114, 0xf>(c); a += dppf<0x118, 0xf>(a); c += dppf<0x118, 0xf>(c);
    a += dppf<0x142, 0xa>(a); c += dppf<0x142, 0xa>(c); a += dppf<0x143, 0xc>(a); c += dppf<0x143, 0xc>(c);
    a = __builtin_bit_cast(float, __builtin_amdgcn_readlane(__builtin_bit_cast(int, a), 63));
    c = __builtin_bit_cast(float, __builtin_amdgcn_readlane(__builtin_bit_cast(int, c), 63));
}
__device__ __forceinline__ f2 unpk(unsigned w) { return f2{bflo(w), bfhi(w)}; }
__device__ __forceinline__ f2 ln_scalars(float sd1, float sd2, float pivot) {
    const float md = sd1 * (1.f / DM);
    const float var = __builtin_fmaxf(__builtin_fmaf(-md, md, sd2 * (1.f / DM)), 0.f);
    const float m = rbf(pivot + md);
    const float sd = rbf(__builtin_amdgcn_sqrtf(rbf(rbf(var) + 1e-5f)));
    const float rs = __builtin_amdgcn_rcpf(sd);
    return f2{rs, -m * rs};
}

struct CF { v2u Al[8], Ah[8]; v4u G[8]; };
template <int P0, int I>
__device__ __forceinline__ void cf_load(CF& f, unsigned ab, unsigned gb) {
    if constexpr (I < 8) {
        f.G[I] = *(LAS const volatile v4u*)(size_t)(gb + I * 512 + (7 - P0) * 64);
    } else {
        constexpr int q = (I - 8) >> 1, p = P0 + 8 * q;
        if constexpr ((I & 1) == 0) f.Al[q] = *(LAS const volatile v2u*)(size_t)(ab + (63 - p) * 64);
        else f.Ah[q] = *(LAS const volatile v2u*)(size_t)(ab + (63 - p) * 64 + 8);
    }
}
template <int P0, int I>
__device__ __forceinline__ void cf_load_all(CF& f, unsigned ab, unsigned gb) {
    if constexpr (I < 24) { cf_load<P0, I>(f, ab, gb); cf_load_all<P0, I + 1>(f, ab, gb); }
}
template <int PN, bool NEXT, bool WR, int M>
__device__ __forceinline__ void cf_step(f4 (&acc)[8], const CF& c, CF& n, unsigned nab, unsigned ngb, const f4 (&old)[8], unsigned wa) {
    if constexpr (M < 36) {
        constexpr int q = M < 8 ? 0 : M < 15 ? 1 : M < 21 ? 2 : M < 26 ? 3 : M < 30 ? 4 : M < 33 ? 5 : M < 35 ? 6 : 7;
        constexpr int st = q == 0 ? 0 : q == 1 ? 8 : q == 2 ? 15 : q == 3 ? 21 : q == 4 ? 26 : q == 5 ? 30 : q == 6 ? 33 : 35;
        constexpr int e = M - st;
        v4u a = {c.Al[q].x, c.Al[q].y, c.Ah[q].x, c.Ah[q].y};
        acc[q + e] = __builtin_amdgcn_mfma_f32_16x16x32_f16(__builtin_bit_cast(h8, a), __builtin_bit_cast(h8, c.G[e]), acc[q + e], 0, 0, 0);
        if constexpr (NEXT && M < 24) cf_load<PN, M>(n, nab, ngb);
        if constexpr (WR && M >= 26 && M < 34) *(LAS volatile f4*)(size_t)(wa + (M - 26) * 1024) = old[M - 26];
        __builtin_amdgcn_sched_barrier(0);
        cf_step<PN, NEXT, WR, M + 1>(acc, c, n, nab, ngb, old, wa);
    }
}
__device__ __forceinline__ void zero8(f4 (&a)[8]) {
#pragma unroll
    for (int i = 0; i < 8; i++) a[i] = f4{0.f, 0.f, 0.f, 0.f};
}

__global__ void __launch_bounds__(NT, 2) fused_kernel(Params p) {
    extern __shared__ __attribute__((aligned(16))) char lds_raw[];
    const int tid = threadIdx.x, lane = tid & 63;
    const int wv = __builtin_amdgcn_readfirstlane(tid >> 6);
    const int b = blockIdx.x;
    const bool conv = b < NG;
    (void)lds_raw;

#define RSRC(ptr, bytes) __builtin_amdgcn_make_buffer_rsrc((void*)(ptr), 0, (int)(bytes), 0x00020000)
    const auto rws = RSRC(p.ws, WS_END);
    const auto rout = RSRC(p.out, SEQ * DM * 2);

    STAMP(0, 0);
    unsigned xw[5];
    {
        const auto rx = RSRC(p.x, SEQ * DM * 2);
#pragma unroll
        for (int i = 0; i < 5; i++) xw[i] = __builtin_amdgcn_raw_buffer_load_b32(rx, lane * 4 + i * 256, (8 * b + wv) * (DM * 2), 0);
    }
    const unsigned ep = __builtin_amdgcn_raw_buffer_load_b32(rws, 0, WS_EPOCH, 16);
    v2u mCr, mCi; unsigned ldt;
    const int scw = wv == 2 ? 0 : (wv == 3 ? 1 : 2);
    unsigned plA[3], pAi[3], pCr[3], pCi[3];
    {
        const auto rlA = RSRC(p.lA, DM * NM * 2), rAi = RSRC(p.Aim, DM * NM * 2), rCr = RSRC(p.Cr, DM * NM * 2), rCi = RSRC(p.Ci, DM * NM * 2);
        int chs = 3 * b + scw; chs = chs < DM ? chs : DM - 1;
        int chl = 3 * b + (lane < 2 ? lane : 2); chl = chl < DM ? chl : DM - 1;
        const int l48 = lane < 48 ? lane : 47;
        mCr = __builtin_amdgcn_raw_buffer_load_b64(rCr, l48 * 8, chs * (NM * 2), 0);
        mCi = __builtin_amdgcn_raw_buffer_load_b64(rCi, l48 * 8, chs * (NM * 2), 0);
        ldt = __builtin_amdgcn_raw_buffer_load_b16(RSRC(p.ldt, DM * 2), chl * 2, 0, 0);
#pragma unroll
        for (int j = 0; j < 3; j++) {
            int rd = (wv >> 1) + 4 * j; rd = rd < 9 ? rd : 8;
            const int c = rd / 3;
            int ch = 3 * b + c; ch = ch < DM ? ch : DM - 1;
            const int so = (ch * NM + (rd - 3 * c) * 64) * 2;
            plA[j] = __builtin_amdgcn_raw_buffer_load_b16(rlA, lane * 2, so, 0);
            pAi[j] = __builtin_amdgcn_raw_buffer_load_b16(rAi, lane * 2, so, 0);
            pCr[j] = __builtin_amdgcn_raw_buffer_load_b16(rCr, lane * 2, so, 0);
            pCi[j] = __builtin_amdgcn_raw_buffer_load_b16(rCi, lane * 2, so, 0);
        }
    }
    unsigned wrw[5], dsw[5];
    {
        const auto rwr = RSRC(p.wr, DM * 2), rds = RSRC(p.Dsk, DM * 2);
#pragma unroll
        for (int i = 0; i < 5; i++) { dsw[i] = __builtin_amdgcn_raw_buffer_load_b32(rds, lane * 4 + i * 256, 0, 0); wrw[i] = __builtin_amdgcn_raw_buffer_load_b32(rwr, lane * 4 + i * 256, 0, 0); }
    }
    if (tid < 32) LDS(unsigned, L_CTL + 4 * tid) = 0;
    if (tid < 384) LDS(unsigned, L_U + (tid >> 7) * U_CH + (tid & 127) * 4) = 0;
    if (tid < 384) LDS(v4u, L_PART + (tid < 192 ? 3 : 11) * 3072 + (tid % 192) * 16) = v4u{0, 0, 0, 0};
    LDS(unsigned, L_TAB + 6 * TAB_ROW + tid * 4) = 0x00003C00u;
    if (tid < 64) LDS(unsigned, L_TAB + 6 * TAB_ROW + (512 + tid) * 4) = 0x00003C00u;

#define PIN(x) asm volatile("" : "+v"(x))
    const int t7 = tid & 127, t8 = tid & 255;
    unsigned puL = L_ROWS + ((tid & 3) * ROW_DW + 3 * (tid >> 2)) * 4;
    unsigned puO = WS_U + (tid >> 2) * 16384 + (tid & 3) * 16 + b * 64;
    unsigned lnA = L_ROWS + (wv >> 1) * ROW_DW * 4 + lane * 8;
    unsigned kT1, kT2, kM1, kM2, kLa, kLb;
    {
        const unsigned i4 = (lane & 15) * 4;
        const unsigned long long cT1 = 0x5544332211110000ull, cT2 = 0x2020202032103210ull  , cM2 = 0x2121212100000000ull  ,
                                 cLa = 0xBA90BA90BA90BA90ull  , cLb = 0xEEEEDDDDCCCC0000ull  ;
        const unsigned col = L_TAB + (lane & 48) + (9 * (wv & 3) + (wv < 4 ? 0 : 5)) * 64;
        kT1 = col + (unsigned)((cT1 >> i4) & 15) * TAB_ROW;
        kT2 = col + 6 * TAB_ROW + (unsigned)((cT2 >> i4) & 15) * TAB_ROW;
        kM1 = col + 10 * TAB_ROW + ((lane & 8) ? TAB_ROW : 0);
        kM2 = col + 12 * TAB_ROW + (unsigned)((cM2 >> i4) & 15) * TAB_ROW;
        kLa = col + 6 * TAB_ROW + (unsigned)((cLa >> i4) & 15) * TAB_ROW;
        kLb = col + 6 * TAB_ROW + (unsigned)((cLb >> i4) & 15) * TAB_ROW;
    }
    PIN(puL); PIN(puO); PIN(lnA); PIN(kT1); PIN(kT2); PIN(kM1); PIN(kM2); PIN(kLa); PIN(kLb);

    f2 xv[5], uv[5];
    {
        const float pivot = bflo(__builtin_amdgcn_readfirstlane(xw[0]));
        f2 s1 = {0.f, 0.f}, s2 = {0.f, 0.f};
#pragma unroll
        for (int i = 0; i < 5; i++) { xv[i] = unpk(xw[i]); const f2 d = xv[i] - pivot; s1 += d; s2 = __builtin_elementwise_fma(d, d, s2); }
        float a1 = s1.x + s1.y, a2 = s2.x + s2.y;
        wave_sum2(a1, a2);
        const f2 sc = ln_scalars(a1, a2, pivot);
        const unsigned base = lnA + (wv & 1) * 2;
#pragma unroll
        for (int i = 0; i < 5; i++) {
            uv[i] = __builtin_elementwise_fma(xv[i], f2{sc.x, sc.x}, f2{sc.y, sc.y});
            const unsigned uh = pkh(uv[i].x, uv[i].y);
            LDS(u16, base + i * 512) = (u16)uh; LDS(u16, base + i * 512 + 4) = (u16)(uh >> 16);
        }
    }
    STAMP(21, 0);
    BAR();
    STAMP(1, 0);
    asm volatile("" :: "v"(plA[0]), "v"(plA[1]), "v"(plA[2]), "v"(pAi[0]), "v"(pAi[1]), "v"(pAi[2]), "v"(pCr[0]), "v"(pCr[1]), "v"(pCr[2]),
                 "v"(pCi[0]), "v"(pCi[1]), "v"(pCi[2]), "v"(mCr), "v"(mCi), "v"(ldt), "v"(ep));
    asm volatile("" :: "v"(wrw[0]), "v"(wrw[1]), "v"(wrw[2]), "v"(wrw[3]), "v"(wrw[4]), "v"(dsw[0]), "v"(dsw[1]), "v"(dsw[2]), "v"(dsw[3]), "v"(dsw[4]));
    const unsigned tag1 = ep + 1u, tag2 = ep + 0x40000001u;
    {
        const unsigned a0 = puL, a1 = a0 + 3 * 128 * 4;
        const v4u v0 = {LDS(unsigned, a0), LDS(unsigned, a0 + 4), LDS(unsigned, a0 + 8), tag1};
        const unsigned o0 = puO;
        ST_SC1(v0, rws, o0);
        if (tid < NG * 4 - NT) {
            const v4u v1 = {LDS(unsigned, a1), LDS(unsigned, a1 + 4), LDS(unsigned, a1 + 8), tag1};
            ST_SC1(v1, rws, o0 + 128 * 16384);
        }
    }
    STAMP(2, 0);
    float dtv;
    {
        const float ld = bflo(ldt), t = ld * 1.44269504089f;
        const float te = __builtin_fmaf(ld, 1.44269504089f, -t) + ld * 1.925963033e-8f;
        dtv = rbf(__builtin_amdgcn_exp2f(t) * (1.f + te * 0.69314718056f));
    }
    if (wv == 2 || wv == 3 || wv == 6) {
        const unsigned m0 = mCr.x & 0x7fff7fffu, m1 = mCr.y & 0x7fff7fffu, m2 = mCi.x & 0x7fff7fffu, m3 = mCi.y & 0x7fff7fffu;
        unsigned mx = umax(umax(umax(m0 & 0xffffu, m0 >> 16), umax(m1 & 0xffffu, m1 >> 16)), umax(umax(m2 & 0xffffu, m2 >> 16), umax(m3 & 0xffffu, m3 >> 16)));
        mx = wave_max(mx);
        const float dt = __builtin_bit_cast(float, __builtin_amdgcn_readlane(__builtin_bit_cast(int, dtv), scw));
        int ex = (int)((__float_as_uint(bflo(mx) * dt) >> 23) & 0xff);
        ex = ex < 2 ? 125 : (ex > 250 ? 250 : ex);
        const float S = __uint_as_float((unsigned)(252 - ex) << 23);
        if (lane == 0) LDS(f2, L_CTL + C_SC + 8 * scw) = f2{2.f * S * dt, __uint_as_float((unsigned)(ex + 2) << 23)};
    }
    if (wv == 3 && conv && lane < 15) {
        const int gi = b * 15 + lane;
        const unsigned o = gi < 256 ? WS_R + gi * 64 : (gi < 256 + 1536 ? WS_HID + (gi - 256) * 16 : WS_XS + (gi - 1792) * 16);
        if (gi < 1792 + 1344) ST_SC1((v4u{SENT, SENT, SENT, SENT}), rws, o);
    }
    f2 xt[5];
#pragma unroll
    for (int i = 0; i < 5; i++) xt[i] = __builtin_elementwise_fma(uv[i], unpk(dsw[i]), xv[i]);

    v4u wdf[8][4];
    if (conv) {
    if (conv) {
#pragma unroll
        for (int j = 0; j < 3; j++) {
            if (j < 2 || wv < 2) {
                const int rd = (wv >> 1) + 4 * j;
                const float dt = __builtin_bit_cast(float, __builtin_amdgcn_readlane(__builtin_bit_cast(int, dtv), rd / 3));
                const float la = bflo(plA[j]), t = la * 1.44269504089f;
                const float te = __builtin_fmaf(la, 1.44269504089f, -t) + la * 1.925963033e-8f;
                const float ar = rbf(__builtin_amdgcn_exp2f(t) * (1.f + te * 0.69314718056f));
                const f2 z = f2{ar * -1.44269504089f, bflo(pAi[j]) * 0.15915494309f} * dt;
                unsigned a = L_TAB + (rd * 64 + lane) * 4;
                asm("" : "+v"(a));
#define TW(row, v) LDS(unsigned, a + (row) * TAB_ROW) = pkh((v).x, (v).y)
                if ((wv & 1) == 0) {
                    const f2 E1 = cexp_at(z, 1.f), E8 = cexp_at(z, f2{8.f, -8.f});
                    const FF2 p1 = cmulf2(E1, E1, E8, E8);
                    TW(10, E1); TW(15, E8);
                    const FF2 p2 = cmulf2(p1.a, p1.a, p1.b, p1.b);
                    TW(7, p1.a); TW(16, p1.b);
                    const FF2 p3 = cmulf2(p2.a, p1.a, p1.b, E8);
                    TW(8, p2.a); TW(18, p2.b);
                    const f2 E64 = cmulf(p2.b, p2.b);
                    TW(9, p3.a); TW(17, p3.b);
                    const f2 E96 = cmulf(E64, p2.b);
                    TW(19, E64); TW(20, E96);
                } else {
                    const f2 E128 = cexp_at(z, 128.f), E1028 = cexp_at(z, 1028.f);
                    const f2 E256 = cmulf(E128, E128);
                    TW(14, E1028);
                    const f2 E512 = cmulf(E256, E256);
                    TW(12, E256);
                    const f2 E1024 = cmulf(E512, E512);
                    TW(11, E512); TW(13, E1024);
                    float cs;
                    do { cs = LDSV(float, L_CTL + C_SC + 8 * (rd / 3)); } while (cs == 0.f);
                    const f2 C = f2{bflo(pCr[j]), bflo(pCi[j])} * cs;
                    const FF2 p1 = cmulf2(C, E128, C, E512);
                    TW(0, C);
                    const FF2 p3 = cmulf2(p1.b, E128, p1.b, E256);
                    TW(1, p1.a); TW(2, p1.b);
                    const f2 C896 = cmulf(p3.b, E128);
                    TW(3, p3.a); TW(4, p3.b); TW(5, C896);
                }
#undef TW
            }
        }
    }
    unsigned kFlA, kFlB, rdP, rdA, rdB, exA0, exO0, exA1, exO1;
    {
        const int i = lane & 15, g = lane >> 4;
        kFlA = L_PART + ((0xA7519620u >> (4 * wv)) & 15u) * 3072 + lane * 16;
        kFlB = L_PART + (wv == 1 ? 4 : 8) * 3072 + lane * 16;
        const int c = wv < 3 ? wv : wv - 3, a2 = 2 * (g - 2);
        const unsigned wb = L_W + (c < 3 ? c : 0) * W_CH;
        rdP = L_PART + c * 12288 + lane * 16 + (wv < 3 ? 0 : 2048);
        rdA = wb + 2 * (wv < 3 ? (g < 2 ? 128 * g + 8 * i : 384 + 32 * a2 + 2 * i) : (g < 2 ? 256 + 64 * g + 4 * i : 576 + 16 * a2 + i));
        rdB = wb + 2 * (512 + 16 * a2 + i);
        auto exaddr = [&](int task, unsigned& a, unsigned& o) {
            const int cc = task >> 2, part = task & 3, m = 64 * part + lane, J = 2040 - 8 * m;
            const int x = part < 2 ? 512 + ((J - 1024) >> 3) : (part == 2 ? 384 + ((J - 512) >> 2) : (lane < 32 ? 256 + ((J - 256) >> 1) : J));
            a = L_W + cc * W_CH + x * 2; o = L_TAB + cc * KC_CH + m * 16;
        };
        exaddr(wv, exA0, exO0);
        exaddr(wv < 2 ? 8 + wv : (wv >= 6 ? 4 + wv : 0), exA1, exO1);
        PIN(kFlA); PIN(kFlB); PIN(rdP); PIN(rdA); PIN(rdB); PIN(exA0); PIN(exO0); PIN(exA1); PIN(exO1);
    }
    BAR();
    STAMP(3, 0);
    if (conv) {
        struct TabRegs { v4u T1, T2, M1, M2, La, Lb; };
        auto fetch = [&](TabRegs& t, int off) {
            t.T1 = LDSV(v4u, kT1 + off); t.T2 = LDSV(v4u, kT2 + off); t.La = LDSV(v4u, kLa + off);
            t.Lb = LDSV(v4u, kLb + off); t.M1 = LDSV(v4u, kM1 + off); t.M2 = LDSV(v4u, kM2 + off);
        };
        f4 a0 = {0, 0, 0, 0}, a1 = a0, a2 = a0;
        auto step = [&](const TabRegs& t) {
            const v4u H0 = cmulh4(t.T1, t.T2), Lv = cmulh4(t.La, t.Lb);
            const v4u H1 = cmulh4(H0, t.M1);
            a0 = __builtin_amdgcn_mfma_f32_16x16x32_f16(__builtin_bit_cast(h8, H0), __builtin_bit_cast(h8, Lv), a0, 0, 0, 0);
            const v4u H2 = cmulh4(H0, t.M2);
            a1 = __builtin_amdgcn_mfma_f32_16x16x32_f16(__builtin_bit_cast(h8, H1), __builtin_bit_cast(h8, Lv), a1, 0, 0, 0);
            a2 = __builtin_amdgcn_mfma_f32_16x16x32_f16(__builtin_bit_cast(h8, H2), __builtin_bit_cast(h8, Lv), a2, 0, 0, 0);
        };
        auto flush = [&](unsigned pa) {
            LDS(f4, pa) = a0; LDS(f4, pa + 1024) = a1; LDS(f4, pa + 2048) = a2;
            a0 = f4{0, 0, 0, 0}; a1 = a0; a2 = a0;
        };
        TabRegs t0, t1, t2, t3, t4;
        fetch(t0, 0); fetch(t1, 64); fetch(t2, 128); fetch(t3, 192);
        if (wv < 4) fetch(t4, 256);
        __builtin_amdgcn_sched_barrier(0);
        step(t0); if (wv == 6) flush(kFlA);
        step(t1);
        step(t2); if (wv == 1) flush(kFlA);
        step(t3);
        if (wv < 4) step(t4);
        flush(wv == 1 || wv == 6 ? kFlB : kFlA);
    }
    STAMP(16, 0); STAMP(17, 4);
    BAR();
    if (conv && wv < 6) {
        if (wv < 3) {
            const f4 k0 = (LDS(f4, rdP) + LDS(f4, rdP + 3072)) + (LDS(f4, rdP + 6144) + LDS(f4, rdP + 9216));
            const f4 k1 = (LDS(f4, rdP + 1024) + LDS(f4, rdP + 1024 + 3072)) + (LDS(f4, rdP + 1024 + 6144) + LDS(f4, rdP + 1024 + 9216));
            if (lane < 32) {
                LDS(v4u, rdA) = v4u{pkh(k0.x, k1.x), pkh(k0.y, k1.y), pkh(k0.z, k1.z), pkh(k0.w, k1.w)};
            } else {
                LDS(unsigned, rdA) = pkh(k0.x, k0.y);
                LDS(unsigned, rdA + 64) = pkh(k0.z, k0.w);
                const unsigned t = pkh(k1.x, k1.z);
                LDS(u16, rdB) = (u16)t; LDS(u16, rdB + 32) = (u16)(t >> 16);
            }
        } else {
            const f4 k2 = (LDS(f4, rdP) + LDS(f4, rdP + 3072)) + (LDS(f4, rdP + 6144) + LDS(f4, rdP + 9216));
            if (lane < 32) {
                LDS(v2u, rdA) = v2u{pkh(k2.x, k2.y), pkh(k2.z, k2.w)};
            } else {
                const unsigned t = pkh(k2.x, k2.z);
                LDS(u16, rdA) = (u16)t; LDS(u16, rdA + 32) = (u16)(t >> 16);
                if (lane == 63) LDS(u16, rdA + 34) = (u16)(pkh(k2.w, k2.w));
            }
        }
    }
    STAMP(18, 0);
    BAR();
    v4u gu[4];
    if (wv >= 4) {
#pragma unroll
        for (int i = 0; i < 4; i++) gu[i] = __builtin_amdgcn_raw_buffer_load_b128(rws, WS_U + i * 4096 + t8 * 16, b * 16384, 16);
    }
    {
        auto expand = [&](int part, unsigned a, unsigned o) {
            const int J = 2040 - 8 * (64 * part + lane);
            unsigned D0, D2, D4, D6, D8, Da;
            auto two = [](unsigned lo, unsigned hi) { return lo | (hi << 16); };
            if (part < 2) {
                const unsigned vm = LDS(u16, a - 2), v0 = LDS(u16, a), v1 = LDS(u16, a + 2);
                D0 = two(v1, v1); D2 = (J & 8) ? D0 : two(v1, v0); D4 = two(v0, v0); D6 = D4; D8 = D4; Da = J == 1024 ? vm : v0;
            } else if (part == 2) {
                const unsigned dm = LDS(unsigned, a - 4), d0 = LDS(unsigned, a), v2 = LDS(u16, a + 4);
                const unsigned v0 = d0 & 0xffffu, v1 = d0 >> 16, vm2 = dm & 0xffffu, vm1 = dm >> 16;
                D0 = two(v2, v2); D2 = two(v1, v1); D4 = two(v1, v0); D6 = two(v0, v0);
                D8 = J == 512 ? two(v0, vm1) : D6; Da = J == 512 ? vm2 : vm1;
            } else if (lane < 32) {
                const v2u dm = LDS(v2u, a - 8), d = LDS(v2u, a); const unsigned v4 = LDS(u16, a + 8);
                const unsigned v0 = d.x & 0xffffu, v1 = d.x >> 16, v2 = d.y & 0xffffu, v3 = d.y >> 16;
                const unsigned vm3 = dm.x >> 16, vm2 = dm.y & 0xffffu, vm1 = dm.y >> 16;
                D0 = two(v4, v3); D2 = two(v2, v2); D4 = two(v2, v1); D6 = two(v0, v0);
                D8 = J == 256 ? two(vm1, vm2) : two(v0, vm1); Da = J == 256 ? vm3 : vm2;
            } else {
                const v4u d = LDS(v4u, a);
                v2u e = LDS(v2u, a - 8);
                if (J == 0) e = v2u{0, 0};
                D0 = __builtin_amdgcn_alignbit(d.w, d.w, 16); D2 = __builtin_amdgcn_alignbit(d.z, d.z, 16);
                D4 = __builtin_amdgcn_alignbit(d.y, d.y, 16); D6 = __builtin_amdgcn_alignbit(d.x, d.x, 16);
                D8 = __builtin_amdgcn_alignbit(e.y, e.y, 16); Da = e.x >> 16;
            }
            const unsigned D1 = __builtin_amdgcn_alignbit(D2, D0, 16), D3 = __builtin_amdgcn_alignbit(D4, D2, 16), D5 = __builtin_amdgcn_alignbit(D6, D4, 16),
                           D7 = __builtin_amdgcn_alignbit(D8, D6, 16), D9 = __builtin_amdgcn_alignbit(Da, D8, 16);
            LDS(v4u, o) = v4u{D0, D2, D4, D6};
            LDS(v4u, o + KC_COPY) = v4u{D1, D3, D5, D7};
            LDS(v4u, o + 2 * KC_COPY) = v4u{D2, D4, D6, D8};
            LDS(v4u, o + 3 * KC_COPY) = v4u{D3, D5, D7, D9};
        };
        expand(wv & 3, exA0, exO0);
        if (wv < 2 || wv >= 6) expand(wv & 3, exA1, exO1);
        if (tid < 24) LDS(v4u, L_TAB + (tid >> 3) * KC_CH + ((tid >> 1) & 3) * KC_COPY + (256 + (tid & 1)) * 16) = v4u{0, 0, 0, 0};
    }
    STAMP(4, 0);
    if (wv >= 4) {
        for (int spin = 0; spin < SPIN_CAP; spin++) {
            unsigned long long need[4], any = 0;
#pragma unroll
            for (int i = 0; i < 4; i++) { need[i] = __ballot(gu[i].w != tag1); any |= need[i]; }
            if (!any) break;
            __builtin_amdgcn_s_sleep(2);
            asm volatile("" ::: "memory");
#pragma unroll
            for (int i = 0; i < 4; i++)
                if ((quad_or(need[i]) >> lane) & 1) gu[i] = __builtin_amdgcn_raw_buffer_load_b128(rws, WS_U + i * 4096 + t8 * 16, b * 16384, 16);
        }
#pragma unroll
        for (int i = 0; i < 4; i++) {
            const unsigned a = L_U + (128 + i * 256 + t8) * 4;
            LDS(unsigned, a) = gu[i].x; LDS(unsigned, a + U_CH) = gu[i].y; LDS(unsigned, a + 2 * U_CH) = gu[i].z;
        }
    }
    unsigned cvA, cvG, epY, pyL, pyO;
    {
        const int i = lane & 15, g = lane >> 4;
        const int cp = (3 - i) & 3, cw = wv < 3 ? wv : 0;
        cvA = L_TAB + cp * KC_COPY + (15 - i + 8 * g - cp) * 2 + cw * KC_CH;
        cvG = L_U + (16 + 16 * i + 8 * g) * 2 + cw * U_CH;
        epY = L_Y + cw * Y_CH + i * 40 + g * 8;
        const int l = 8 * (t8 >> 2) + 2 * (t8 & 3);
        pyL = L_Y + (l >> 4) * 40 + (l & 15) * 2;
        pyO = WS_Y + (t8 >> 2) * (NG * 64) + (t8 & 3) * 16 + b * 64;
        PIN(cvA); PIN(cvG); PIN(epY); PIN(pyL); PIN(pyO);
    }
    BAR();
    STAMP(5, 0);
    f4 acc[8];
    if (conv && wv < 4) {
        const unsigned aoff = cvA, goff = cvG;
        CF f0, f1;
        STAMP(28, 0); CSTAMP(29, 0);
        if (wv < 3) {
            const unsigned ab = aoff, gb = goff;
            zero8(acc);
            cf_load_all<0, 0>(f0, ab, gb);
            __builtin_amdgcn_sched_barrier(0);
            cf_step<1, true, false, 0>(acc, f0, f1, ab, gb, acc, 0);
            cf_step<2, true, false, 0>(acc, f1, f0, ab, gb, acc, 0);
            cf_step<3, true, false, 0>(acc, f0, f1, ab, gb, acc, 0);
            cf_step<4, true, false, 0>(acc, f1, f0, ab, gb, acc, 0);
            cf_step<5, true, false, 0>(acc, f0, f1, ab, gb, acc, 0);
            cf_step<6, false, false, 0>(acc, f1, f0, ab, gb, acc, 0);
        } else {
            VMWAIT();
            f4 alt[8];
            const unsigned pw = L_P + lane * 16;
            zero8(acc); zero8(alt);
            cf_load_all<6, 0>(f0, aoff, goff);
            __builtin_amdgcn_sched_barrier(0);
            cf_step<7, true, false, 0>(acc, f0, f1, aoff, goff, alt, 0);
            cf_step<6, true, false, 0>(acc, f1, f0, aoff + KC_CH, goff + U_CH, alt, 0);
            cf_step<7, true, true, 0>(alt, f0, f1, aoff + KC_CH, goff + U_CH, acc, pw);
            cf_step<6, true, false, 0>(alt, f1, f0, aoff + 2 * KC_CH, goff + 2 * U_CH, acc, 0);
            zero8(acc);
            cf_step<7, true, true, 0>(acc, f0, f1, aoff + 2 * KC_CH, goff + 2 * U_CH, alt, pw + 8192);
            cf_step<7, false, false, 0>(acc, f1, f0, 0, 0, alt, 0);
#pragma unroll
            for (int i = 0; i < 8; i++) LDS(f4, pw + 16384 + i * 1024) = acc[i];
        }
    } else if (wv == 4 && b == 0 && lane == 0) {
        __builtin_amdgcn_raw_buffer_store_b32(tag1, rws, 0, WS_EPOCH, 16);
    }
    STAMP(22, 0); CSTAMP(30, 0); STAMP(23, 3);
    BAR();
    STAMP(6, 0);
    if (b == 0 && tid == 7 * 64) __builtin_amdgcn_raw_buffer_store_b32(tag2, rws, 0, WS_FLAG, 16);
    if (conv && wv < 3) {
        const float is = LDS(float, L_CTL + C_SC + 8 * wv + 4);
        f4 pp[8];
#pragma unroll
        for (int i = 0; i < 8; i++) pp[i] = LDS(f4, L_P + wv * 8192 + i * 1024 + lane * 16);
#pragma unroll
        for (int i = 0; i < 8; i++) {
            const f4 s = (acc[i] + pp[i]) * is;
            LDS(v2u, epY + i * 640) = v2u{pkbf(s.x, s.y), pkbf(s.z, s.w)};
        }
    }
    STAMP(24, 0);
    BAR();
    if (wv < 4) {
            const unsigned a = pyL, o = pyO;
            v4u v[4];
#pragma unroll
            for (int i = 0; i < 4; i++) v[i] = v4u{LDS(unsigned, a + i * 1280), LDS(unsigned, a + i * 1280 + Y_CH), LDS(unsigned, a + i * 1280 + 2 * Y_CH), tag2};
#pragma unroll
            for (int i = 0; i < 4; i++) ST_SC1(v[i], rws, o + i * 64 * (NG * 64));
            STAMP(7, 0);
    }
    } else if (wv != 4 && wv != 5) {
        const int wi = wv < 4 ? wv : wv - 2;
        if (b < F2_0) {
            const auto rw = wi < 3 ? RSRC(p.wg, DM * FF * 2) : RSRC(p.wu, DM * FF * 2);
            const int i0 = (wi < 3 ? wi : wi - 3) * 20;
#pragma unroll
            for (int i = 0; i < 20; i++) {
                const int c = (i0 + i) * 64 + lane, row = c / 6;
                dma16(rw, row * (FF * 2) + (c - 6 * row) * 16, (b - F1_0) * 96, L_FW + (wi < 3 ? 0 : FW_IMG) + (i0 + i) * 1024);
            }
            VMWAIT();
        } else {
            const auto rw = RSRC(p.wd, FF * DM * 2);
            const unsigned st = L_FW + wi * 16384;
            const unsigned ta = st + (8 * (lane >> 4) + ((lane & 15) >> 2)) * 128 + (lane & 3) * 8;
#pragma unroll
            for (int h = 0; h < 2; h++) {
#pragma unroll
                for (int i = 0; i < 16; i++) dma16(rw, (lane >> 3) * (DM * 2) + (lane & 7) * 16, (256 * wi + 128 * h + 8 * i) * (DM * 2) + (b - F2_0) * 128, st + i * 1024);
                VMWAIT();
#pragma unroll
                for (int k = 0; k < 4; k++)
#pragma unroll
                    for (int ct = 0; ct < 4; ct++) {
                        const v2u u0 = __builtin_bit_cast(v2u, __builtin_amdgcn_ds_read_tr16_b64_v4i16((LAS s4*)(size_t)(ta + k * 4096 + ct * 32)));
                        const v2u u1 = __builtin_bit_cast(v2u, __builtin_amdgcn_ds_read_tr16_b64_v4i16((LAS s4*)(size_t)(ta + k * 4096 + ct * 32 + 512)));
                        wdf[4 * h + k][ct] = v4u{u0.x, u0.y, u1.x, u1.y};
                    }
                LGWAIT();
            }
        }
    }
    {
        const unsigned ybase = WS_Y + b * (NG * 64);
        auto sweep = [&](auto nld, int t, int nthr) {
            constexpr int N = decltype(nld)::value;
            v4u gy[N];
            const bool last = t < NG * 4 - (N - 1) * nthr;
#pragma unroll
            for (int i = 0; i < N; i++) gy[i] = LD_SC1(rws, ybase + (i * nthr + (i < N - 1 || last ? t : 0)) * 16);
            for (int spin = 0; spin < SPIN_CAP; spin++) {
                unsigned long long need[N], any = 0;
#pragma unroll
                for (int i = 0; i < N; i++) { need[i] = __ballot(gy[i].w != tag2); any |= need[i]; }
                if (!any) break;
                __builtin_amdgcn_s_sleep(RS2);
                asm volatile("" ::: "memory");
#pragma unroll
                for (int i = 0; i < N; i++)
                    if ((quad_or(need[i]) >> lane) & 1) gy[i] = LD_SC1(rws, ybase + (i * nthr + (i < N - 1 || last ? t : 0)) * 16);
            }
            const unsigned a = L_ROWS + ((t & 3) * ROW_DW + 3 * (t >> 2)) * 4;
#pragma unroll
            for (int i = 0; i < N; i++) {
                if (i < N - 1 || last) { const unsigned ai = a + i * (nthr * 3); LDS(unsigned, ai) = gy[i].x; LDS(unsigned, ai + 4) = gy[i].y; LDS(unsigned, ai + 8) = gy[i].z; }
            }
        };
        if (conv) {
            if (wv >= 4) { __builtin_amdgcn_s_sleep(DLY_S2); sweep(IC<4>{}, t8, 256); }
        } else if (wv == 4 || wv == 5) {
            for (int spin = 0; spin < SPIN_CAP; spin++) {
                asm volatile("" ::: "memory");
                if (__builtin_amdgcn_readfirstlane(__builtin_amdgcn_raw_buffer_load_b32(rws, 0, WS_FLAG, 16)) == tag2) break;
                __builtin_amdgcn_s_sleep(DLY_PROBE);
            }
            __builtin_amdgcn_s_sleep(DLY_S2B);
            sweep(IC<7>{}, t7, 128);
        }
    }
    auto ffn1_frags = [&]() {
        const unsigned wa = L_FW + (8 * (lane >> 4) + ((lane & 15) >> 2)) * 96 + (lane & 3) * 8 + wv * 3072;
#pragma unroll
        for (int j = 0; j < 3; j++) {
            if (j < 2 || wv < 4) {
#pragma unroll
                for (int t = 0; t < 6; t++) {
                    const unsigned o = wa + j * 8 * 3072 + (t < 3 ? t * 32 : FW_IMG + (t - 3) * 32);
                    const v2u u0 = __builtin_bit_cast(v2u, __builtin_amdgcn_ds_read_tr16_b64_v4i16((LAS s4*)(size_t)o));
                    const v2u u1 = __builtin_bit_cast(v2u, __builtin_amdgcn_ds_read_tr16_b64_v4i16((LAS s4*)(size_t)(o + 384)));
                    wdf[(6 * j + t) >> 2][(6 * j + t) & 3] = v4u{u0.x, u0.y, u1.x, u1.y};
                }
            }
        }
    };
    f2 wr2[5];
#pragma unroll
    for (int i = 0; i < 5; i++) wr2[i] = unpk(wrw[i]);
    STAMP(25, 4);
    BAR();
    STAMP(8, 0);
    f2 hv[5], x2[5];
    unsigned rbits;
    {
        const unsigned a = lnA;
        const unsigned sel2 = (wv & 1) ? 0x03020c0cu : 0x01000c0cu;
        v2u d[5];
#pragma unroll
        for (int i = 0; i < 5; i++) d[i] = LDS(v2u, a + i * 512);
#pragma unroll
        for (int i = 0; i < 5; i++) hv[i] = xt[i] + f2{__uint_as_float(__builtin_amdgcn_perm(0u, d[i].x, sel2)), __uint_as_float(__builtin_amdgcn_perm(0u, d[i].y, sel2))};
        const float pivot = __builtin_bit_cast(float, __builtin_amdgcn_readfirstlane(__builtin_bit_cast(int, hv[0].x)));
        f2 s1 = {0.f, 0.f}, s2 = {0.f, 0.f};
#pragma unroll
        for (int i = 0; i < 5; i++) { const f2 e = hv[i] - pivot; s1 += e; s2 = __builtin_elementwise_fma(e, e, s2); }
        float a1 = s1.x + s1.y, a2 = s2.x + s2.y;
        wave_sum2(a1, a2);
        const f2 sc = ln_scalars(a1, a2, pivot);
        f2 rr = {0.f, 0.f};
#pragma unroll
        for (int i = 0; i < 5; i++) { x2[i] = __builtin_elementwise_fma(hv[i], f2{sc.x, sc.x}, f2{sc.y, sc.y}); rr = __builtin_elementwise_fma(x2[i], wr2[i], rr); }
        const float r = wave_sum(rr.x + rr.y);
        rbits = pkbf(r, r) & 0xffffu;
        STAMP(26, 0);
        if (lane == 0) LDS(u16, L_CTL + C_R8 + 2 * wv) = (u16)rbits;
    }
    BAR();
    if (wv == 7) {
        if (lane == 0) { const v4u r = LDS(v4u, L_CTL + C_R8); ST_SC1((v4u{nosent(r.x), nosent(r.y), nosent(r.z), nosent(r.w)}), rws, WS_R + b * 64); }
        STAMP(9, 7);
    }
    if (wv < 4) {
        __builtin_amdgcn_s_sleep(DLY_S3);
        v4u rr = LD_SC1(rws, WS_R + tid * 64);
        for (int spin = 0; spin < SPIN_CAP; spin++) {
            const bool need = rr.x == SENT || rr.y == SENT || rr.z == SENT || rr.w == SENT;
            if (!__any(need)) break;
            __builtin_amdgcn_s_sleep(1);
            asm volatile("" ::: "memory");
            if (need) rr = LD_SC1(rws, WS_R + tid * 64);
        }
        STAMP(10, 0);
        unsigned comp[8], mx = 0;
        const unsigned ib = 2047u - 8u * tid;
#pragma unroll
        for (int j = 0; j < 4; j++) {
            const unsigned w = rr[j], sg = (w >> 15) & 0x00010001u;
            const unsigned k = w ^ (0x80008000u | ((sg << 15) - sg));
            comp[2 * j] = (k << 16) | (ib - 2 * j);
            comp[2 * j + 1] = (k & 0xffff0000u) | (ib - 2 * j - 1);
            mx = umax(mx, umax(comp[2 * j], comp[2 * j + 1]));
        }
        mx = umax(mx, dppu<0x111, 0xf>(mx)); mx = umax(mx, dppu<0x112, 0xf>(mx)); mx = umax(mx, dppu<0x114, 0xf>(mx));
        unsigned tau = (unsigned)__builtin_amdgcn_readlane((int)mx, 7);
#pragma unroll
        for (int g = 1; g < 8; g++) { const unsigned t = (unsigned)__builtin_amdgcn_readlane((int)mx, 8 * g + 7); tau = t < tau ? t : tau; }
        unsigned base = 0;
        const unsigned cb = L_CAND + wv * 2048;
#pragma unroll
        for (int e = 0; e < 8; e++) {
            const bool is = comp[e] >= tau;
            const unsigned long long mk = __ballot(is);
            const unsigned pos = base + __builtin_amdgcn_mbcnt_hi((unsigned)(mk >> 32), __builtin_amdgcn_mbcnt_lo((unsigned)mk, 0));
            LDS(unsigned, is ? cb + 4 * pos : L_CTL + C_DUMP) = comp[e];
            base += __popcll(mk);
        }
        if (lane == 0) LDS(unsigned, L_CTL + C_GMAX + 4 * wv) = base;
    }
    if (b >= F1_0 && b < F2_0 && wv >= 4) {
        ffn1_frags();
    }
    STAMP(27, 0);
    BAR();
    const unsigned mytok = 8 * b + wv;
    int myrank;
    {
        const v4u n = LDS(v4u, L_CTL + C_GMAX);
        const unsigned sg = rbits >> 15;
        const unsigned me = (((rbits ^ (0x8000u | (sg ? 0x7fffu : 0u))) & 0xffffu) << 16) | (2047u - mytok);
        unsigned c[4];
#pragma unroll
        for (int r = 0; r < 4; r++) c[r] = LDSV(unsigned, L_CAND + r * 2048 + 4 * lane);
        int cnt = 0;
#pragma unroll
        for (int r = 0; r < 4; r++) cnt += __popcll(__ballot(c[r] > me && (unsigned)lane < n[r]));
        const unsigned nmax = umax(umax(n.x, n.y), umax(n.z, n.w));
        if (__builtin_amdgcn_readfirstlane(nmax) > 64) {
#pragma unroll
            for (int r = 0; r < 4; r++)
                for (unsigned j0 = 64; j0 < (n[r] & 0x3ffu); j0 += 64) {
                    const unsigned cc = j0 + lane < n[r] ? LDS(unsigned, L_CAND + r * 2048 + 4 * (j0 + lane)) : 0u;
                    cnt += __popcll(__ballot(cc > me));
                }
        }
        myrank = cnt < 8 ? cnt : -1;
    }
    STAMP(11, 0);
    if (myrank >= 0) {
        const unsigned st = L_STG + wv * 2560;
#pragma unroll
        for (int i = 0; i < 5; i++) {
            LDS(unsigned, st + i * 256 + lane * 4) = nosent(pkbf(x2[i].x, x2[i].y));
            LDS(unsigned, st + 1280 + i * 256 + lane * 4) = nosent(pkbf(hv[i].x, hv[i].y));
        }
        LGWAIT();
        const unsigned dst = WS_XS + myrank * XS_STRIDE;
        ST_SC1(LDS(v4u, st + lane * 16), rws, dst + lane * 16);
        ST_SC1(LDS(v4u, st + 1024 + lane * 16), rws, dst + 1024 + lane * 16);
        if (lane < 32) ST_SC1(LDS(v4u, st + 2048 + lane * 16), rws, dst + 2048 + lane * 16);
        if (lane == 32) ST_SC1((v4u{mytok, rbits, 0u, 0u}), rws, dst + 2560);
    }
    if (b >= F1_0 && b < F2_0 && wv < 4) ffn1_frags();
    auto store_out = [&]() {
        if (myrank < 0) {
            const unsigned st = L_FW + wv * 1280;
#pragma unroll
            for (int i = 0; i < 5; i++) { const f2 o = hv[i] + x2[i]; LDS(unsigned, st + i * 256 + lane * 4) = pkbf(o.x, o.y); }
            LGWAIT();
            const v4u v0 = LDS(v4u, st + lane * 16);
            __builtin_amdgcn_raw_buffer_store_b128(v0, rout, lane * 16, mytok * (DM * 2), OUT_AUX);
            if (lane < 16) { const v4u v1 = LDS(v4u, st + 1024 + lane * 16); __builtin_amdgcn_raw_buffer_store_b128(v1, rout, 1024 + lane * 16, mytok * (DM * 2), OUT_AUX); }
        }
    };
    if (conv) store_out();
    auto has_sent = [](v4u a) { return a.x == SENT || a.y == SENT || a.z == SENT || a.w == SENT; };
    if (b >= F1_0 && b < F2_0) {
        const int g = lane >> 4, q = (lane & 15) >> 2, pp = lane & 3;
        if (wv == 4 || wv == 5) {
            __builtin_amdgcn_s_sleep(DLY_XS);
            v4u a[5];
            unsigned off[5];
#pragma unroll
            for (int i = 0; i < 5; i++) { const int q5 = t7 + 128 * i; off[i] = WS_XS + (q5 / 80) * XS_STRIDE + (q5 % 80) * 16; a[i] = LD_SC1(rws, off[i]); }
            for (int spin = 0; spin < SPIN_CAP; spin++) {
                bool need[5], any = false;
#pragma unroll
                for (int i = 0; i < 5; i++) { need[i] = has_sent(a[i]); any |= need[i]; }
                if (!__any(any)) break;
                __builtin_amdgcn_s_sleep(1);
                asm volatile("" ::: "memory");
#pragma unroll
                for (int i = 0; i < 5; i++) if (need[i]) a[i] = LD_SC1(rws, off[i]);
            }
#pragma unroll
            for (int i = 0; i < 5; i++) { const int q5 = t7 + 128 * i; LDS(v4u, L_XS + (q5 / 80) * XS_ROW + (q5 % 80) * 16) = a[i]; }
        }
        BAR();
        STAMP(12, 0);
        auto red1 = [](int w) { return w < 3 ? L_ROWS + 3072 * w : (w < 5 ? L_CAND + 3072 * (w - 3) : L_SP + 3072 * (w - 5)); };
        {
            f4 d[6];
#pragma unroll
            for (int t = 0; t < 6; t++) d[t] = f4{0, 0, 0, 0};
            const unsigned xa = L_XS + (lane & 7) * XS_ROW + g * 16 + wv * 64;
            const v4u av0 = LDS(v4u, xa), av1 = LDS(v4u, xa + 512);
            v4u av2 = av0;
            if (wv < 4) av2 = LDS(v4u, xa + 1024);
#pragma unroll
            for (int t = 0; t < 6; t++) d[t] = __builtin_amdgcn_mfma_f32_16x16x32_bf16(__builtin_bit_cast(b8, av0), __builtin_bit_cast(b8, wdf[t >> 2][t & 3]), d[t], 0, 0, 0);
#pragma unroll
            for (int t = 0; t < 6; t++) d[t] = __builtin_amdgcn_mfma_f32_16x16x32_bf16(__builtin_bit_cast(b8, av1), __builtin_bit_cast(b8, wdf[(6 + t) >> 2][(6 + t) & 3]), d[t], 0, 0, 0);
            if (wv < 4) {
#pragma unroll
                for (int t = 0; t < 6; t++) d[t] = __builtin_amdgcn_mfma_f32_16x16x32_bf16(__builtin_bit_cast(b8, av2), __builtin_bit_cast(b8, wdf[(12 + t) >> 2][(12 + t) & 3]), d[t], 0, 0, 0);
            }
            if (lane < 32) {
                const unsigned ro = red1(wv) + lane * 16;
#pragma unroll
                for (int t = 0; t < 6; t++) LDS(f4, ro + t * 512) = d[t];
            }
        }
        BAR();
        if (wv < 6) {
            const int c = lane >> 3, e = lane & 7;
            const unsigned ra = (wv >> 1) * 512 + ((c >> 2) * 16 + 8 * (wv & 1) + e) * 16 + (c & 3) * 4;
            float gp[8], up[8];
#pragma unroll
            for (int w = 0; w < 8; w++) { gp[w] = LDS(float, red1(w) + ra); up[w] = LDS(float, red1(w) + ra + 1536); }
            const float gs = rbf(((gp[0] + gp[1]) + (gp[2] + gp[3])) + ((gp[4] + gp[5]) + (gp[6] + gp[7])));
            const float us = rbf(((up[0] + up[1]) + (up[2] + up[3])) + ((up[4] + up[5]) + (up[6] + up[7])));
            const float sl = rbf(gs * __builtin_amdgcn_rcpf(1.f + __builtin_amdgcn_exp2f(gs * -1.44269504089f)));
            const unsigned hb = pkbf(sl * us, 0.f) & 0xffffu;
            const unsigned w0 = hb | (dppu<0x101, 0xf>(hb) << 16);
            const unsigned w1_ = dppu<0x102, 0xf>(w0), w2 = dppu<0x104, 0xf>(w0), w3 = dppu<0x106, 0xf>(w0);
            if (e == 0) ST_SC1((v4u{nosent(w0), nosent(w1_), nosent(w2), nosent(w3)}), rws, WS_HID + (6 * (b - F1_0) + wv) * 128 + c * 16);
            STAMP(13, 0);
        }
        store_out();
    }
    if (b >= F2_0) {
        const int w2 = b - F2_0;
        v2u meta; unsigned ex2, eh;
        {
            __builtin_amdgcn_s_sleep(DLY_XS);
            const unsigned sl = WS_XS + wv * XS_STRIDE, co = (64 * w2 + (lane & 62)) * 2;
            for (int spin = 0; spin < SPIN_CAP; spin++) {
                asm volatile("" ::: "memory");
                meta = __builtin_amdgcn_raw_buffer_load_b64(rws, sl + 2560, 0, 16);
                ex2 = __builtin_amdgcn_raw_buffer_load_b32(rws, sl + co, 0, 16);
                eh = __builtin_amdgcn_raw_buffer_load_b32(rws, sl + 1280 + co, 0, 16);
                if (!__any(meta.x == SENT || meta.y == SENT || ex2 == SENT || eh == SENT)) break;
                __builtin_amdgcn_s_sleep(4);
            }
        }
        STAMP(19, 0);
        f4 d[4] = {{0, 0, 0, 0}, {0, 0, 0, 0}, {0, 0, 0, 0}, {0, 0, 0, 0}};
        const int wi = wv < 4 ? wv : wv - 2;
        if (wv == 4 || wv == 5) store_out();
        if (wv != 4 && wv != 5) {
            __builtin_amdgcn_s_sleep(DLY_HID);
            v4u a[8];
            const unsigned ho = WS_HID + ((32 * wi + (lane >> 4)) * 8 + (lane & 7)) * 16;
#pragma unroll
            for (int j = 0; j < 8; j++) a[j] = LD_SC1(rws, ho + j * 512);
            for (int spin = 0; spin < SPIN_CAP; spin++) {
                bool need[8], any = false;
#pragma unroll
                for (int j = 0; j < 8; j++) { need[j] = has_sent(a[j]); any |= need[j]; }
                if (!__any(any)) break;
                __builtin_amdgcn_s_sleep(2);
                asm volatile("" ::: "memory");
#pragma unroll
                for (int j = 0; j < 8; j++) if (need[j]) a[j] = LD_SC1(rws, ho + j * 512);
            }
            STAMP(20, 0);
            store_out();
#pragma unroll
            for (int j = 0; j < 8; j++)
#pragma unroll
                for (int ct = 0; ct < 4; ct++)
                    d[ct] = __builtin_amdgcn_mfma_f32_16x16x32_bf16(__builtin_bit_cast(b8, a[j]), __builtin_bit_cast(b8, wdf[j][ct]), d[ct], 0, 0, 0);
            if (lane < 32) {
#pragma unroll
                for (int ct = 0; ct < 4; ct++) LDS(f4, L_RED + wi * 2048 + ct * 512 + lane * 16) = d[ct];
            }
        }
        BAR();
        STAMP(14, 0);
        {
            const unsigned ra = L_RED + (lane >> 4) * 512 + ((wv >> 2) * 16 + (lane & 15)) * 16 + (wv & 3) * 4;
            float yp[6];
#pragma unroll
            for (int w = 0; w < 6; w++) yp[w] = LDS(float, ra + w * 2048);
            float y = ((yp[0] + yp[1]) + (yp[2] + yp[3])) + (yp[4] + yp[5]);
            const float sg = rbf(__builtin_amdgcn_rcpf(1.f + __builtin_amdgcn_exp2f(bflo(meta.y) * -1.44269504089f)));
            y = rbf(rbf(y) * sg);
            const float o = ((lane & 1) ? bfhi(eh) : bflo(eh)) + rbf(((lane & 1) ? bfhi(ex2) : bflo(ex2)) + y);
            __builtin_amdgcn_raw_buffer_store_b16((u16)pkbf(o, o), rout, (meta.x * DM + 64 * w2 + lane) * 2, 0, OUT_AUX);
        }
    }
}

}

extern "C" void kernel_launch(void* const* d_in, const int* in_sizes, int n_in, void* d_out, int out_size, void* d_ws, size_t ws_size,
                              hipStream_t stream) {
    (void)in_sizes; (void)n_in; (void)out_size; (void)ws_size;
    Params p;
    p.x = (const u16*)d_in[0]; p.lA = (const u16*)d_in[1]; p.Aim = (const u16*)d_in[2]; p.Cr = (const u16*)d_in[3]; p.Ci = (const u16*)d_in[4];
    p.ldt = (const u16*)d_in[5]; p.Dsk = (const u16*)d_in[6]; p.wr = (const u16*)d_in[8]; p.wg = (const u16*)d_in[9]; p.wu = (const u16*)d_in[10];
    p.wd = (const u16*)d_in[11]; p.out = (u16*)d_out; p.ws = (char*)d_ws;
    (void)hipFuncSetAttribute((const void*)fused_kernel, hipFuncAttributeMaxDynamicSharedMemorySize, (int)L_END);
    fused_kernel<<<NWG, NT, L_END, stream>>>(p);
}
```
